```python
import torch

def _softmax(x, axis=-1):
    return torch.softmax(x, dim=axis)

def _gelu(x):
    return torch.nn.functional.gelu(x, approximate='tanh')

def _silu(x):
    return torch.nn.functional.silu(x)

def _sigmoid(x):
    return torch.sigmoid(x)

def _layernorm(x, axis=-1, eps=1e-05):
    m = x.mean(dim=axis, keepdim=True)
    v = x.var(dim=axis, keepdim=True, unbiased=False)
    return (x - m) / torch.sqrt(v + eps)

def _scatter_add(tgt, idx, src, axis=0):
    return tgt.clone().index_add_(axis, idx, src)

def _segment_sum(src, seg, n):
    out = torch.zeros((n,) + src.shape[1:], dtype=src.dtype)
    return out.index_add_(0, seg, src)

def _pad(x, pw):
    flat = tuple((v for pair in reversed(pw) for v in pair))
    return torch.nn.functional.pad(x, flat)
kernel_name = 'standalone__autoregressive_conv_wavernn__speech'

def setup_inputs():

    _w = lambda a: (lambda t: t.to(torch.bfloat16) if t.is_floating_point() else t)(torch.as_tensor(a))
    B, T, D, K = (2, 1000, 512, 5)
    return {'x': _w((lambda *s: torch.randn(*s, dtype=torch.bfloat16) * 0.05)(B, T, D)), 'w': _w((lambda *s: torch.randn(*s, dtype=torch.bfloat16) * 0.05)(K, D, 2 * D))}

def reference(x, w):
    B, T, D = x.shape
    K = w.shape[0]
    dil = 2
    pad = (K - 1) * dil
    xp = _pad(x, ((0, 0), (pad, 0), (0, 0)))
    h = torch.einsum('btd,de->bte', xp[:, 0:T, :], w[0])
    for i in range(1, K):
        h = h + torch.einsum('btd,de->bte', xp[:, i * dil:i * dil + T, :], w[i])
    f, g = (h[..., :D].clone(), h[..., D:].clone())
    return torch.tanh(f) * _sigmoid(g)
```

```cpp
#include <hip/hip_runtime.h>
#include <hip/hip_bf16.h>
#include <stdint.h>

typedef short s16x8 __attribute__((ext_vector_type(8)));
typedef short s16x4 __attribute__((ext_vector_type(4)));
typedef float f32x16 __attribute__((ext_vector_type(16)));
typedef unsigned u32x4 __attribute__((ext_vector_type(4)));
typedef unsigned u32x2 __attribute__((ext_vector_type(2)));
#define LAS __attribute__((address_space(3)))

constexpr int kB = 2, kT = 1000, kD = 512, kTaps = 5;
constexpr int kN2 = 2 * kD;
constexpr int BM = 128;
constexpr int XROWS = BM + 8;
constexpr int XBUF = XROWS * 128;
constexpr int XPIECES = XROWS / 8;
constexpr int WSLOT = 64 * 128;
constexpr int RING = 12;
constexpr int NCHUNK = kD / 64;
constexpr int NUNIT = NCHUNK * kTaps;
constexpr int LDS_X = 0;
constexpr int LDS_W = 2 * XBUF;
constexpr int LDS_TOTAL = LDS_W + RING * WSLOT;
constexpr int NCONS = 4, NWL = 2;
constexpr int NTHREADS = (NCONS + NWL + 1) * 64;

#define GLDS(src, dst) __builtin_amdgcn_global_load_lds((const unsigned*)(src), (unsigned*)(dst), 16, 0, 0)
#define CFENCE() asm volatile("" ::: "memory")
#define BARRIER() do { CFENCE(); __builtin_amdgcn_s_barrier(); CFENCE(); } while (0)

template <int N> __device__ __forceinline__ void wait_vm() {
    asm volatile("s_waitcnt vmcnt(%0)" :: "n"(N) : "memory");
}

__device__ __forceinline__ unsigned pack_bf16(float a, float b) {
    __hip_bfloat16 x = __float2bfloat16(a), y = __float2bfloat16(b);
    return (unsigned)__builtin_bit_cast(unsigned short, x) | ((unsigned)__builtin_bit_cast(unsigned short, y) << 16);
}

struct Frags { s16x8 wf[2][2]; s16x8 xf[2][2]; };

__global__ void __launch_bounds__(NTHREADS, 2)
wavernn_conv_kernel(const __hip_bfloat16* __restrict__ x, const __hip_bfloat16* __restrict__ w,
                    __hip_bfloat16* __restrict__ out) {
    __shared__ __attribute__((aligned(1024))) unsigned char lds[LDS_TOTAL];
    const int tid = threadIdx.x;
    const int wave = __builtin_amdgcn_readfirstlane(tid >> 6);
    const int lane = tid & 63;

    const int bid = blockIdx.x;
    const int xg = bid & 7, j = bid >> 3;
    const int ntile = (xg & 3) * 4 + (j & 3);
    const int mtile = j >> 2;
    const int batch = xg >> 2;
    const int t0 = mtile * BM;
    const int n0 = ntile * 32;
    const int rot = (j + xg) & 7;

    if (wave < NCONS) {
        const int mt = wave & 1, kq = wave >> 1;
        const int c = lane & 31, h = lane >> 5;
        const int blk = (lane >> 4) & 1, q = (lane & 15) >> 2, p = lane & 3;
        const int posf = (2 * blk + (p >> 1)) ^ ((q >> 1) << 2);
        const unsigned wbase_f = LDS_W + (8 * h + q) * 128 + posf * 16 + 8 * (p & 1);
        const unsigned wbase_g = LDS_W + (8 * h + q) * 128 + (posf ^ 4) * 16 + 8 * (p & 1);
        const unsigned xrowbase = LDS_X + (64 * mt + c) * 128;
        unsigned char* L = lds;

        auto read_frags = [&](Frags& f, int u) {
            const int s = u / kTaps, i = u % kTaps, slot = u % RING, xb = s & 1;
#pragma unroll
            for (int ks2 = 0; ks2 < 2; ++ks2) {
                const int ks = 2 * kq + ks2;
#pragma unroll
                for (int et = 0; et < 2; ++et) {
                    const unsigned a = (et ? wbase_g : wbase_f) + slot * WSLOT + ks * 2048;
                    s16x4 lo = __builtin_amdgcn_ds_read_tr16_b64_v4i16((LAS s16x4*)(L + a));
                    s16x4 hi = __builtin_amdgcn_ds_read_tr16_b64_v4i16((LAS s16x4*)(L + a + 512));
                    f.wf[ks2][et] = __builtin_shufflevector(lo, hi, 0, 1, 2, 3, 4, 5, 6, 7);
                }
                const unsigned pos = (unsigned)((2 * ks + h) ^ (((c >> 1) + i) & 7));
                const unsigned a = xrowbase + xb * XBUF + 2 * i * 128 + pos * 16;
#pragma unroll
                for (int tt = 0; tt < 2; ++tt)
                    f.xf[ks2][tt] = *(const s16x8*)(L + a + tt * 4096);
            }
        };

        f32x16 acc[2][2];
#pragma unroll
        for (int a = 0; a < 2; ++a)
#pragma unroll
            for (int b = 0; b < 2; ++b)
#pragma unroll
                for (int r = 0; r < 16; ++r) acc[a][b][r] = 0.f;

        auto mma = [&](const Frags& f) {
#pragma unroll
            for (int ks2 = 0; ks2 < 2; ++ks2)
#pragma unroll
                for (int et = 0; et < 2; ++et)
#pragma unroll
                    for (int tt = 0; tt < 2; ++tt)
                        acc[et][tt] = __builtin_amdgcn_mfma_f32_32x32x16_bf16(f.wf[ks2][et], f.xf[ks2][tt], acc[et][tt], 0, 0, 0);
        };

        Frags fr[2];
        BARRIER();
        read_frags(fr[0], 0);
#pragma unroll
        for (int u = 0; u < NUNIT; ++u) {
            BARRIER();
            if (u + 1 < NUNIT) read_frags(fr[(u + 1) & 1], u + 1);
            mma(fr[u & 1]);
        }
        {
            unsigned char* mine = L + LDS_W + ((NUNIT + wave) % RING) * WSLOT;
            const int ott = kq ^ 1;
#pragma unroll
            for (int et = 0; et < 2; ++et)
#pragma unroll
                for (int g4 = 0; g4 < 4; ++g4) {
                    u32x4 v;
#pragma unroll
                    for (int r = 0; r < 4; ++r) v[r] = __builtin_bit_cast(unsigned, ott ? acc[et][1][4 * g4 + r] : acc[et][0][4 * g4 + r]);
                    *(u32x4*)(mine + ((et * 4 + g4) * 64 + lane) * 16) = v;
                }
        }
        asm volatile("s_waitcnt lgkmcnt(0)" ::: "memory");
        BARRIER();
        {
            const unsigned char* theirs = L + LDS_W + ((NUNIT + (wave ^ 2)) % RING) * WSLOT;
            float fv[16], gv[16];
#pragma unroll
            for (int g4 = 0; g4 < 4; ++g4) {
                u32x4 a = *(const u32x4*)(theirs + ((0 * 4 + g4) * 64 + lane) * 16);
                u32x4 b = *(const u32x4*)(theirs + ((1 * 4 + g4) * 64 + lane) * 16);
#pragma unroll
                for (int r = 0; r < 4; ++r) {
                    const unsigned au = a[r], bu = b[r];
                    fv[4 * g4 + r] = (kq ? acc[0][1][4 * g4 + r] : acc[0][0][4 * g4 + r]) + __uint_as_float(au);
                    gv[4 * g4 + r] = (kq ? acc[1][1][4 * g4 + r] : acc[1][0][4 * g4 + r]) + __uint_as_float(bu);
                }
            }
            const int t = t0 + 32 * (2 * mt + kq) + c;
            if (t < kT) {
                __hip_bfloat16* o = out + ((size_t)(batch * kT + t)) * kD + n0 + 4 * h;
#pragma unroll
                for (int g4 = 0; g4 < 4; ++g4) {
                    float o4[4];
#pragma unroll
                    for (int r = 0; r < 4; ++r) {
                        const float f = fv[4 * g4 + r], g = gv[4 * g4 + r];
                        o4[r] = tanhf(f) * (1.0f / (1.0f + __expf(-g)));
                    }
                    u32x2 pk; pk[0] = pack_bf16(o4[0], o4[1]); pk[1] = pack_bf16(o4[2], o4[3]);
                    *(u32x2*)(o + 8 * g4) = pk;
                }
            }
        }
    } else if (wave < NCONS + NWL) {
        const int lw = wave - NCONS;
        const int dr0 = 32 * lw + (lane >> 3);
        const int chunk = (lane & 7) ^ (((lane >> 4) & 1) << 2);
        const int col = (chunk < 4) ? (n0 + 8 * chunk) : (kD + n0 + 8 * (chunk - 4));
        const __hip_bfloat16* gl = w + (size_t)dr0 * kN2 + col;
        auto issue = [&](int u) {
            const int s = u / kTaps, i = u % kTaps, slot = u % RING;
            const int sr = (s + rot) & 7;
            const __hip_bfloat16* g = gl + (size_t)(i * kD + 64 * sr) * kN2;
#pragma unroll
            for (int pp = 0; pp < 4; ++pp)
                GLDS(g + pp * 8 * kN2, lds + LDS_W + slot * WSLOT + (4 * lw + pp) * 1024);
        };
#pragma unroll
        for (int u = 0; u < RING; ++u) issue(u);
        wait_vm<4 * (RING - 1)>();
        BARRIER();
#pragma unroll
        for (int u = 0; u < NUNIT; ++u) {
            constexpr int dummy = 0; (void)dummy;
            {
                const int issued_max = (u >= 2) ? ((u - 2 + RING < NUNIT - 1) ? (u - 2 + RING) : (NUNIT - 1)) : (RING - 1);
                const int allowed = (u + 1 <= NUNIT - 1) ? 4 * (issued_max - (u + 1)) : 0;
                switch (allowed) {
#define WC(n) case n: wait_vm<n>(); break;
                    WC(0) WC(4) WC(8) WC(12) WC(16) WC(20) WC(24) WC(28) WC(32) WC(36) WC(40) WC(44) WC(48)
#undef WC
                    default: wait_vm<0>(); break;
                }
            }
            BARRIER();
            if (u >= 1 && u - 1 + RING < NUNIT) issue(u - 1 + RING);
        }
        BARRIER();
    } else {
        const int rsub = lane >> 3;
        auto issue = [&](int s) {
            const int sr = (s + rot) & 7;
#pragma unroll
            for (int pc = 0; pc < XPIECES; ++pc) {
                if (pc == 0 && t0 == 0) continue;
                const int rr = 8 * pc + rsub;
                int t = t0 - 8 + rr; t = t < kT ? t : kT - 1;
                const int cp = (lane & 7) ^ ((rr >> 1) & 7);
                const __hip_bfloat16* g = x + ((size_t)(batch * kT + t)) * kD + 64 * sr + cp * 8;
                GLDS(g, lds + LDS_X + (s & 1) * XBUF + pc * 1024);
            }
        };
        if (t0 == 0) {
            u32x4 z = {0u, 0u, 0u, 0u};
            *(u32x4*)(lds + LDS_X + lane * 16) = z;
            *(u32x4*)(lds + LDS_X + XBUF + lane * 16) = z;
        }
        issue(0);
        issue(1);
        if (t0 == 0) wait_vm<XPIECES - 1>(); else wait_vm<XPIECES>();
        asm volatile("s_waitcnt lgkmcnt(0)" ::: "memory");
        BARRIER();
#pragma unroll
        for (int u = 0; u < NUNIT; ++u) {
            const int s = u / kTaps, i = u % kTaps;
            if (i == kTaps - 1) wait_vm<0>();
            BARRIER();
            if (i == 0 && s >= 1 && s + 1 < NCHUNK) issue(s + 1);
        }
        BARRIER();
    }
}

extern "C" void kernel_launch(void* const* d_in, const int* in_sizes, int n_in,
                              void* d_out, int out_size, void* d_ws, size_t ws_size,
                              hipStream_t stream) {
    const __hip_bfloat16* x = (const __hip_bfloat16*)d_in[0];
    const __hip_bfloat16* w = (const __hip_bfloat16*)d_in[1];
    __hip_bfloat16* out = (__hip_bfloat16*)d_out;
    wavernn_conv_kernel<<<256, NTHREADS, 0, stream>>>(x, w, out);
}
```

```cpp
#include <hip/hip_runtime.h>
#include <hip/hip_bf16.h>
#include <stdint.h>

typedef short s16x8 __attribute__((ext_vector_type(8)));
typedef short s16x4 __attribute__((ext_vector_type(4)));
typedef float f32x16 __attribute__((ext_vector_type(16)));
typedef unsigned u32x4 __attribute__((ext_vector_type(4)));
typedef unsigned u32x2 __attribute__((ext_vector_type(2)));
#define LAS __attribute__((address_space(3)))

constexpr int kB = 2, kT = 1000, kD = 512, kTaps = 5;
constexpr int kN2 = 2 * kD;
constexpr int BM = 64;
constexpr int XROWS = BM + 8;
constexpr int XBUF = XROWS * 128;
constexpr int XPIECES = XROWS / 8;
constexpr int WSLOT = 64 * 256;
constexpr int RING = kTaps;
constexpr int NCHUNK = kD / 64;
constexpr int NUNIT = NCHUNK * kTaps;
constexpr int LDS_W = 0;
constexpr int LDS_X = RING * WSLOT;
constexpr int LDS_TOTAL = LDS_X + 2 * XBUF;
constexpr int NCONS = 4, NLD = 8;
constexpr int NTHREADS = (NCONS + NLD) * 64;

#define BLDS(rsrc, voff, soff, dst) __builtin_amdgcn_raw_ptr_buffer_load_lds((rsrc), (LAS void*)(dst), 16, (voff), (soff), 0, 0)
#define CFENCE() asm volatile("" ::: "memory")
#define BARRIER() do { CFENCE(); __builtin_amdgcn_s_barrier(); CFENCE(); } while (0)

template <int N> __device__ __forceinline__ void wait_vm() {
    asm volatile("s_waitcnt vmcnt(%0)" :: "n"(N) : "memory");
}

__device__ __forceinline__ unsigned pack_bf16(float a, float b) {
    __hip_bfloat16 x = __float2bfloat16(a), y = __float2bfloat16(b);
    return (unsigned)__builtin_bit_cast(unsigned short, x) | ((unsigned)__builtin_bit_cast(unsigned short, y) << 16);
}

struct Frags { s16x8 wf[2][2]; s16x8 xf[2][2]; };

__global__ void __launch_bounds__(NTHREADS, 3)
wavernn_conv_kernel(const __hip_bfloat16* __restrict__ x, const __hip_bfloat16* __restrict__ w,
                    __hip_bfloat16* __restrict__ out) {
    __shared__ __attribute__((aligned(1024))) unsigned char lds[LDS_TOTAL];
    const int tid = threadIdx.x;
    const int wave = __builtin_amdgcn_readfirstlane(tid >> 6);
    const int lane = tid & 63;

    const int bid = blockIdx.x;
    const int xg = bid & 7, j = bid >> 3;
    const int ntile = (xg >> 1) * 2 + (j & 1);
    const int mtile = j >> 1;
    const int batch = xg & 1;
    const int t0 = mtile * BM;
    const int n0 = ntile * 64;
    const int rot = (j + xg) & 7;

    if (wave < NCONS) {
        const int eh = wave & 1, kq = wave >> 1;
        const int c = lane & 31, h = lane >> 5;
        const int blk = (lane >> 4) & 1, q = (lane & 15) >> 2, p = lane & 3;
        const int posf = (4 * eh + 2 * blk + (p >> 1)) ^ (q << 2);
        const unsigned wbase_f = LDS_W + (8 * h + q) * 256 + posf * 16 + 8 * (p & 1) + kq * 8192;
        const unsigned wbase_g = LDS_W + (8 * h + q) * 256 + (posf ^ 8) * 16 + 8 * (p & 1) + kq * 8192;
        const unsigned xrowbase = LDS_X + c * 128;
        unsigned char* L = lds;

        auto read_frags = [&](Frags& f, int i, int xb) {
#pragma unroll
            for (int ks2 = 0; ks2 < 2; ++ks2) {
#pragma unroll
                for (int et = 0; et < 2; ++et) {
                    const unsigned a = (et ? wbase_g : wbase_f) + i * WSLOT + ks2 * 4096;
                    s16x4 lo = __builtin_amdgcn_ds_read_tr16_b64_v4i16((LAS s16x4*)(L + a));
                    s16x4 hi = __builtin_amdgcn_ds_read_tr16_b64_v4i16((LAS s16x4*)(L + a + 1024));
                    f.wf[ks2][et] = __builtin_shufflevector(lo, hi, 0, 1, 2, 3, 4, 5, 6, 7);
                }
                const unsigned pos = (unsigned)((4 * kq + 2 * ks2 + h) ^ (((c >> 1) + i) & 7));
                const unsigned a = xrowbase + xb * XBUF + 2 * i * 128 + pos * 16;
#pragma unroll
                for (int tt = 0; tt < 2; ++tt)
                    f.xf[ks2][tt] = *(const s16x8*)(L + a + tt * 4096);
            }
        };

        f32x16 acc[2][2];
#pragma unroll
        for (int a = 0; a < 2; ++a)
#pragma unroll
            for (int b = 0; b < 2; ++b)
#pragma unroll
                for (int r = 0; r < 16; ++r) acc[a][b][r] = 0.f;

        auto mma = [&](const Frags& f) {
#pragma unroll
            for (int ks2 = 0; ks2 < 2; ++ks2)
#pragma unroll
                for (int et = 0; et < 2; ++et)
#pragma unroll
                    for (int tt = 0; tt < 2; ++tt)
                        acc[et][tt] = __builtin_amdgcn_mfma_f32_32x32x16_bf16(f.wf[ks2][et], f.xf[ks2][tt], acc[et][tt], 0, 0, 0);
        };

        Frags fr[2];
        BARRIER();
        read_frags(fr[0], 0, 0);
        for (int cc = 0; cc < NCHUNK / 2; ++cc) {
#pragma unroll
            for (int ul = 0; ul < 2 * kTaps; ++ul) {
                const int i = ul % kTaps, c2 = ul / kTaps;
                const int ni = (i + 1) % kTaps, nxb = (i == kTaps - 1) ? (c2 ^ 1) : c2;
                BARRIER();
                __builtin_amdgcn_sched_barrier(0);
                read_frags(fr[(ul + 1) & 1], ni, nxb);
                mma(fr[ul & 1]);
#pragma unroll
                for (int k = 0; k < 6; ++k) {
                    __builtin_amdgcn_sched_group_barrier(0x008, 1, 0);
                    __builtin_amdgcn_sched_group_barrier(0x100, 2, 0);
                }
                __builtin_amdgcn_sched_group_barrier(0x008, 2, 0);
                __builtin_amdgcn_sched_barrier(0);
            }
        }
        {
            unsigned char* mine = L + LDS_W + WSLOT + wave * 8192;
            const int ott = kq ^ 1;
#pragma unroll
            for (int et = 0; et < 2; ++et)
#pragma unroll
                for (int g4 = 0; g4 < 4; ++g4) {
                    u32x4 v;
#pragma unroll
                    for (int r = 0; r < 4; ++r) v[r] = __float_as_uint(ott ? acc[et][1][4 * g4 + r] : acc[et][0][4 * g4 + r]);
                    *(u32x4*)(mine + ((et * 4 + g4) * 64 + lane) * 16) = v;
                }
        }
        asm volatile("s_waitcnt lgkmcnt(0)" ::: "memory");
        BARRIER();
        {
            const unsigned char* theirs = L + LDS_W + WSLOT + (wave ^ 2) * 8192;
            float fv[16], gv[16];
#pragma unroll
            for (int g4 = 0; g4 < 4; ++g4) {
                u32x4 a = *(const u32x4*)(theirs + ((0 * 4 + g4) * 64 + lane) * 16);
                u32x4 b = *(const u32x4*)(theirs + ((1 * 4 + g4) * 64 + lane) * 16);
#pragma unroll
                for (int r = 0; r < 4; ++r) {
                    const unsigned au = a[r], bu = b[r];
                    fv[4 * g4 + r] = (kq ? acc[0][1][4 * g4 + r] : acc[0][0][4 * g4 + r]) + __uint_as_float(au);
                    gv[4 * g4 + r] = (kq ? acc[1][1][4 * g4 + r] : acc[1][0][4 * g4 + r]) + __uint_as_float(bu);
                }
            }
            const int t = t0 + 32 * kq + c;
            if (t < kT) {
                __hip_bfloat16* o = out + ((size_t)(batch * kT + t)) * kD + n0 + 32 * eh + 4 * h;
#pragma unroll
                for (int g4 = 0; g4 < 4; ++g4) {
                    float o4[4];
#pragma unroll
                    for (int r = 0; r < 4; ++r) {
                        const float f = fv[4 * g4 + r], g = gv[4 * g4 + r];
                        const float ef = __expf(2.0f * f), eg = __expf(-g);
                        const float th = 1.0f - 2.0f * __builtin_amdgcn_rcpf(1.0f + ef);
                        o4[r] = th * __builtin_amdgcn_rcpf(1.0f + eg);
                    }
                    u32x2 pk; pk[0] = pack_bf16(o4[0], o4[1]); pk[1] = pack_bf16(o4[2], o4[3]);
                    *(u32x2*)(o + 8 * g4) = pk;
                }
            }
        }
    } else {
        const int l = wave - NCONS;
        const int rowp = lane >> 4;
        const int wchunk = (lane & 15) ^ (rowp << 2);
        const int wcol = (wchunk < 8) ? (n0 + 8 * wchunk) : (kD + n0 + 8 * (wchunk - 8));
        const __amdgpu_buffer_rsrc_t rw = __builtin_amdgcn_make_buffer_rsrc((void*)w, 0, kTaps * kD * kN2 * 2, 0x00020000);
        const __amdgpu_buffer_rsrc_t rx = __builtin_amdgcn_make_buffer_rsrc((void*)x, 0, kB * kT * kD * 2, 0x00020000);
        const int wvoff = ((8 * l + rowp) * kN2 + wcol) * 2;
        auto issue_w = [&](int u) {
            const int s = u / kTaps, i = u - s * kTaps;
            const int sr = (s + rot) & 7;
            const int so = (i * kD + 64 * sr) * kN2 * 2;
            unsigned char* dst = lds + LDS_W + i * WSLOT + 2 * l * 1024;
            BLDS(rw, wvoff, so, dst);
            BLDS(rw, wvoff, so + 4 * kN2 * 2, dst + 1024);
        };
        auto issue_x1 = [&](int s, int pc) {
            const int sr = (s + rot) & 7;
            const int rr = 8 * pc + (lane >> 3);
            int t = t0 - 8 + rr; t = t < kT ? t : kT - 1;
            const int cp = (lane & 7) ^ ((rr >> 1) & 7);
            BLDS(rx, ((batch * kT + t) * kD + cp * 8) * 2, __builtin_amdgcn_readfirstlane(64 * sr * 2), lds + LDS_X + (s & 1) * XBUF + pc * 1024);
        };
        auto issue_x = [&](int s) {
            if (l != 0 || t0 != 0) issue_x1(s, l);
            if (l == 0) issue_x1(s, 8);
        };
        if (l == 0 && t0 == 0) {
            u32x4 z = {0u, 0u, 0u, 0u};
            *(u32x4*)(lds + LDS_X + lane * 16) = z;
            *(u32x4*)(lds + LDS_X + XBUF + lane * 16) = z;
            asm volatile("s_waitcnt lgkmcnt(0)" ::: "memory");
        }
        issue_x(0);
        issue_w(0); issue_w(1); issue_w(2); issue_w(3);
        wait_vm<6>();
        BARRIER();
        for (int u = 0; u < NUNIT - 4; ++u) {
            wait_vm<4>();
            BARRIER();
            const int s = u / kTaps;
            if (u - s * kTaps == 0 && s + 1 < NCHUNK) issue_x(s + 1);
            issue_w(u + 4);
        }
        wait_vm<4>(); BARRIER();
        wait_vm<2>(); BARRIER();
        wait_vm<0>(); BARRIER();
        BARRIER();
        BARRIER();
    }
}

extern "C" void kernel_launch(void* const* d_in, const int* in_sizes, int n_in,
                              void* d_out, int out_size, void* d_ws, size_t ws_size,
                              hipStream_t stream) {
    const __hip_bfloat16* x = (const __hip_bfloat16*)d_in[0];
    const __hip_bfloat16* w = (const __hip_bfloat16*)d_in[1];
    __hip_bfloat16* out = (__hip_bfloat16*)d_out;
    wavernn_conv_kernel<<<256, NTHREADS, 0, stream>>>(x, w, out);
}
```
